```python
import torch

def _softmax(x, axis=-1):
    return torch.softmax(x, dim=axis)
def _gelu(x):
    return torch.nn.functional.gelu(x, approximate="tanh")
def _silu(x):
    return torch.nn.functional.silu(x)
def _sigmoid(x):
    return torch.sigmoid(x)
def _layernorm(x, axis=-1, eps=1e-5):
    m = x.mean(dim=axis, keepdim=True)
    v = x.var(dim=axis, keepdim=True, unbiased=False)
    return (x - m) / torch.sqrt(v + eps)
def _scatter_add(tgt, idx, src, axis=0):
    return tgt.clone().index_add_(axis, idx, src)
def _segment_sum(src, seg, n):
    out = torch.zeros((n,) + src.shape[1:], dtype=src.dtype)
    return out.index_add_(0, seg, src)
def _pad(x, pw):
    flat = tuple(v for pair in reversed(pw) for v in pair)
    return torch.nn.functional.pad(x, flat)

kernel_name = 'standalone__paged_block_table_kv_pagedattention__decode'

def setup_inputs():

    _w = (lambda a: (lambda t: t.to(torch.bfloat16) if t.is_floating_point() else t)(torch.as_tensor(a)))
    B, Hq, Hkv, Sq, Skv, D = 2, 32, 32, 1, 8192, 128
    bs = 16
    nbps = max(1, Skv // bs)
    num_phys = B * nbps + 4
    bt = torch.randperm(num_phys)[: B * nbps].reshape(B, nbps).to(torch.int64)
    cl = (nbps * bs - (torch.arange(1, B + 1, dtype=torch.int64) * 2654435761) % max(1, nbps * bs // 2)).to(torch.int64)
    return {
        "q": _w((lambda *s: torch.randn(*s, dtype=torch.bfloat16) * 0.8)(B, Hq, Sq, D)),
        "k_cache": _w((lambda *s: torch.randn(*s, dtype=torch.bfloat16) * 0.8)(num_phys, bs, Hkv, D)),
        "v_cache": _w((lambda *s: torch.randn(*s, dtype=torch.bfloat16) * 0.05)(num_phys, bs, Hkv, D)),
        "block_table": _w(bt),
        "context_lens": _w(cl),
    }

def reference(q, k_cache, v_cache, block_table, context_lens):
    B, Hq, Sq, D = q.shape
    bs = k_cache.shape[1]
    Hkv = k_cache.shape[2]
    nbps = block_table.shape[1]
    Skv = nbps * bs
    bt = block_table.reshape(-1)
    k = (lambda x, *d: x.permute(*d))(k_cache[bt].reshape(B, Skv, Hkv, D), 0, 2, 1, 3)
    v = (lambda x, *d: x.permute(*d))(v_cache[bt].reshape(B, Skv, Hkv, D), 0, 2, 1, 3)
    g = Hq // Hkv
    qg = q.reshape(B, Hkv, g, Sq, D)
    scores = torch.einsum("bhgqd,bhkd->bhgqk", qg, k) * (D ** -0.5)
    pos = torch.arange(Skv)
    msk = pos[None, :] < context_lens[:, None]
    scores = torch.where(msk[:, None, None, None, :], scores, -1e9)
    p = _softmax(scores, axis=-1)
    out = torch.einsum("bhgqk,bhkd->bhgqd", p, v)
    return out.reshape(B, Hq, Sq, D)
```

```cpp
#include <hip/hip_runtime.h>
#include <hip/hip_bf16.h>
#include <cstdio>
#include <cstdint>

using bf16 = __hip_bfloat16;
constexpr int D = 128, PAGE = 16, WAVES = 4, NTHR = WAVES * 64;
constexpr float SCALE = 0.088388347648318440f;
#define ATTN_B 2
#define ATTN_HQ 32
#define ATTN_HKV 32
#define ATTN_CTX 8192
#define ATTN_NSPLIT 16
#define ATTN_COMBINE 0
#define ATTN_KV_NT 1
#define ATTN_PAGED 1
#define ATTN_SQ 1
constexpr int B = ATTN_B, HQ = ATTN_HQ, HKV = ATTN_HKV, CTX = ATTN_CTX, G = HQ / HKV, SQ = ATTN_SQ;
constexpr int GE = G * SQ;
constexpr int GEP = GE <= 4 ? 4 : 8;
constexpr int NBPS = CTX / PAGE;
constexpr int NUM_PHYS = ATTN_PAGED ? B * NBPS + 4 : B * NBPS;
constexpr int nsplit_rule() { int n = 1; while (B * HKV * n < 512 && NBPS / (2 * n) >= 16) n *= 2; return n; }
constexpr int NSPLIT = ATTN_NSPLIT ? ATTN_NSPLIT : nsplit_rule();
constexpr int PPS = NBPS / NSPLIT, PPW = PPS / WAVES;
constexpr int COMBINE = ATTN_COMBINE, KV_NT = ATTN_KV_NT, PAGED = ATTN_PAGED;
constexpr int HPL = GEP / 4;
constexpr int TOK_STRIDE = PAGED ? HKV * D : D;
static_assert(HQ % HKV == 0 && GE >= 1 && GE <= 8, "GE = (HQ/HKV)*SQ in 1..8 (padded to 4 or 8 lane-layout rows); 16 would need acc[128] + x[64] per lane: > 256 VGPRs in this layout");
static_assert(CTX % (PAGE * NSPLIT * WAVES) == 0 && PPW >= 1 && PPW <= 64, "pages split evenly over splits and waves; <= 64 pages per wave (block_table slice in one VGPR)");
constexpr size_t WS_CNT = 0, CNT_BYTES = (size_t)B * HKV * 4, WS_PML = 65536;
constexpr size_t WS_PO = WS_PML + (size_t)B * HQ * SQ * NSPLIT * 2 * 4, WS_END = WS_PO + (size_t)B * HQ * SQ * NSPLIT * D * 4;
constexpr int LDS_P = 0, LDS_MRG = WAVES * 64 * HPL, MRG_REC = 4 + D, LDS_FLAG = LDS_MRG + WAVES * GEP * MRG_REC, LDS_FLOATS = LDS_FLAG + 4;

using bf16x2 = __attribute__((ext_vector_type(2))) __bf16;
using u32x4  = __attribute__((ext_vector_type(4))) unsigned;
using f32x4  = __attribute__((ext_vector_type(4))) float;
using f32x8  = __attribute__((ext_vector_type(8))) float;
__device__ __forceinline__ float dot2(unsigned k, unsigned q, float acc) {
  return __builtin_amdgcn_fdot2_f32_bf16(__builtin_bit_cast(bf16x2, k), __builtin_bit_cast(bf16x2, q), acc, false);
}
__device__ __forceinline__ unsigned cvtpk(float lo, float hi) {
  unsigned r; asm volatile("v_cvt_pk_bf16_f32 %0, %1, %2" : "=v"(r) : "v"(lo), "v"(hi)); return r;
}
template <int CTRL> __device__ __forceinline__ float dpp(float x) {
  return __builtin_bit_cast(float, __builtin_amdgcn_mov_dpp(__builtin_bit_cast(int, x), CTRL, 0xf, 0xf, true));
}
constexpr int XOR1 = 0xB1, XOR2 = 0x4E, XOR7 = 0x141, XOR8 = 0x128, ROR4 = 0x124;
__device__ __forceinline__ float xrow16_max(float x) {
  auto s = __builtin_amdgcn_permlane16_swap(__float_as_uint(x), __float_as_uint(x), false, false);
  x = fmaxf(__uint_as_float(s[0]), __uint_as_float(s[1]));
  auto t = __builtin_amdgcn_permlane32_swap(__float_as_uint(x), __float_as_uint(x), false, false);
  return fmaxf(__uint_as_float(t[0]), __uint_as_float(t[1]));
}
__device__ __forceinline__ float xrow16_sum(float x) {
  auto s = __builtin_amdgcn_permlane16_swap(__float_as_uint(x), __float_as_uint(x), false, false);
  x = __uint_as_float(s[0]) + __uint_as_float(s[1]);
  auto t = __builtin_amdgcn_permlane32_swap(__float_as_uint(x), __float_as_uint(x), false, false);
  return __uint_as_float(t[0]) + __uint_as_float(t[1]);
}
template <typename TIn> struct Stage;
template <> struct Stage<bf16> { using T = u32x4;
  __device__ static __forceinline__ T ld8(const bf16* p) { return KV_NT ? __builtin_nontemporal_load(reinterpret_cast<const u32x4*>(p)) : *reinterpret_cast<const u32x4*>(p); }
  __device__ static __forceinline__ T ld8q(const bf16* p) { return *reinterpret_cast<const u32x4*>(p); }
  __device__ static __forceinline__ u32x4 tobf(T x) { return x; } };
template <> struct Stage<float> { using T = f32x8;
  __device__ static __forceinline__ T ld8(const float* p) { return KV_NT ? __builtin_nontemporal_load(reinterpret_cast<const f32x8*>(p)) : *reinterpret_cast<const f32x8*>(p); }
  __device__ static __forceinline__ T ld8q(const float* p) { return *reinterpret_cast<const f32x8*>(p); }
  __device__ static __forceinline__ u32x4 tobf(T x) { u32x4 w = {cvtpk(x[0], x[1]), cvtpk(x[2], x[3]), cvtpk(x[4], x[5]), cvtpk(x[6], x[7])}; return w; } };

template <typename TIn> struct DecArgs { const TIn* q; const TIn* k; const TIn* v; const int* bt; const int* lens; bf16* out; unsigned char* ws; };

__device__ __forceinline__ void st_partial(float2* p, float2 v) {
  if constexpr (COMBINE == 1) __hip_atomic_store(reinterpret_cast<unsigned long long*>(p), __builtin_bit_cast(unsigned long long, v), __ATOMIC_RELAXED, __HIP_MEMORY_SCOPE_AGENT);
  else *p = v;
}
__device__ __forceinline__ void merge_partials(const float* pml, const float* po, int n, int d0, bf16* o) {
  float M = -INFINITY;
  for (int s = 0; s < n; ++s) M = fmaxf(M, pml[2 * s]);
  float L = 0.f, o0 = 0.f, o1 = 0.f;
  for (int s = 0; s < n; ++s) {
    const float ls = pml[2 * s + 1], w = (ls > 0.f) ? __builtin_amdgcn_exp2f(pml[2 * s] - M) : 0.f;
    const float2 a = *reinterpret_cast<const float2*>(po + (size_t)s * D + d0);
    L = fmaf(w, ls, L); o0 = fmaf(w, a.x, o0); o1 = fmaf(w, a.y, o1);
  }
  const float inv = (L > 0.f) ? 1.f / L : 0.f;
  *reinterpret_cast<unsigned*>(o + d0) = cvtpk(o0 * inv, o1 * inv);
}

template <typename TIn>
__global__ void __launch_bounds__(NTHR) attn_decode_split(DecArgs<TIn> a) {
  __shared__ __attribute__((aligned(16))) float lds[LDS_FLOATS];
  const int s = blockIdx.x, kvh = blockIdx.y, b = blockIdx.z;
  const int tid = threadIdx.x, lane = tid & 63, wave = __builtin_amdgcn_readfirstlane(tid >> 6), r = lane >> 4, c = lane & 15;
  constexpr float C2 = SCALE * 1.4426950408889634f;
  const int len = PAGED ? a.lens[b] : CTX;
  const int npages = (len + PAGE - 1) / PAGE;
  const int pps = (npages + NSPLIT - 1) / NSPLIT;
  const int p0 = s * pps, p1 = min(p0 + pps, npages);
  const int nwp = (p1 > p0 + wave) ? min(PPW, (p1 - p0 - wave + WAVES - 1) / WAVES) : 0;
  int btv = 0;
  if (lane < PPW) { const int pg = min(p0 + wave + 4 * lane, NBPS - 1); btv = PAGED ? a.bt[(size_t)b * NBPS + pg] : pg; }
  u32x4 qpk[GEP];
#pragma unroll
  for (int g = 0; g < GEP; ++g) qpk[g] = g < GE ? Stage<TIn>::tobf(Stage<TIn>::ld8q(a.q + ((size_t)b * HQ * SQ + kvh * GE + g) * D + 8 * c)) : u32x4{0u, 0u, 0u, 0u};
  const size_t loff = (size_t)r * TOK_STRIDE + 8 * c;
#define PAGE_BASE(phys) (PAGED ? (size_t)(phys) * (PAGE * HKV * D) + (size_t)kvh * D : ((size_t)(b * HKV + kvh) * CTX + (size_t)(phys) * PAGE) * D)
#define ISSUE(i_) do { const int phys_ = __builtin_amdgcn_readlane(btv, (i_)); const TIn* kb_ = a.k + PAGE_BASE(phys_) + loff; const TIn* vb_ = a.v + PAGE_BASE(phys_) + loff; \
    _Pragma("unroll") for (int t_ = 0; t_ < 4; ++t_) { Kn[t_] = Stage<TIn>::ld8(kb_ + (size_t)t_ * 4 * TOK_STRIDE); Vn[t_] = Stage<TIn>::ld8(vb_ + (size_t)t_ * 4 * TOK_STRIDE); } } while (0)
  typename Stage<TIn>::T Kn[4], Vn[4];
  float m[HPL], l[HPL], acc[GEP * 8];
#pragma unroll
  for (int j = 0; j < HPL; ++j) { m[j] = -INFINITY; l[j] = 0.f; }
#pragma unroll
  for (int k = 0; k < GEP * 8; ++k) acc[k] = 0.f;
  if (nwp > 0) ISSUE(0);
  for (int i = 0; i < nwp; ++i) {
    u32x4 Kc[4], Vc[4];
#pragma unroll
    for (int t = 0; t < 4; ++t) { Kc[t] = Stage<TIn>::tobf(Kn[t]); Vc[t] = Stage<TIn>::tobf(Vn[t]); }
    const int pg = p0 + wave + 4 * i, rem = len - pg * PAGE;
    if (i + 1 < nwp) ISSUE(i + 1);
    float x[4 * GEP];
#pragma unroll
    for (int t = 0; t < 4; ++t)
#pragma unroll
      for (int g = 0; g < GEP; ++g) { float d = 0.f;
        if (g < GE) {
#pragma unroll
          for (int k = 0; k < 4; ++k) d = dot2(Kc[t][k], qpk[g][k], d); }
        x[t * GEP + g] = d; }
#define TR_STEP(HALF, CTRL, BIT) _Pragma("unroll") for (int h = 0; h < (HALF); ++h) _Pragma("unroll") for (int j = 0; j < HPL; ++j) { \
      const float lo_ = x[h * HPL + j], hi_ = x[(h + (HALF)) * HPL + j]; const float t1_ = lo_ + dpp<CTRL>(lo_), t2_ = hi_ + dpp<CTRL>(hi_); \
      x[h * HPL + j] = (c & (BIT)) ? t2_ : t1_; }
    TR_STEP(8, XOR8, 8) TR_STEP(4, XOR7, 4) TR_STEP(2, XOR2, 2) TR_STEP(1, XOR1, 1)
#undef TR_STEP
    const bool tail = rem < PAGE;
    const bool live = !tail || (r + 4 * (c >> 2)) < rem;
    float p[HPL], alpha[HPL];
#pragma unroll
    for (int j = 0; j < HPL; ++j) {
      const float sc = live ? x[j] * C2 : -INFINITY;
      float pm = sc; pm = fmaxf(pm, dpp<ROR4>(pm)); pm = fmaxf(pm, dpp<XOR8>(pm)); pm = xrow16_max(pm);
      const float mn = fmaxf(m[j], pm), msub = (mn == -INFINITY) ? 0.f : mn;
      alpha[j] = __builtin_amdgcn_exp2f(m[j] - msub); p[j] = __builtin_amdgcn_exp2f(sc - msub);
      l[j] = fmaf(l[j], alpha[j], p[j]); m[j] = mn;
    }
#pragma unroll
    for (int q4 = 0; q4 < 4; ++q4)
#pragma unroll
      for (int j = 0; j < HPL; ++j) { if (q4 * HPL + j >= GE) continue;
        const float al = q4 == 0 ? dpp<0x00>(alpha[j]) : q4 == 1 ? dpp<0x55>(alpha[j]) : q4 == 2 ? dpp<0xAA>(alpha[j]) : dpp<0xFF>(alpha[j]);
#pragma unroll
        for (int k = 0; k < 8; ++k) acc[(q4 * HPL + j) * 8 + k] *= al; }
    float* prow = lds + LDS_P + (wave * 4 + r) * 16 * HPL;
#pragma unroll
    for (int j = 0; j < HPL; ++j) prow[c * HPL + j] = p[j];
    asm volatile("s_waitcnt lgkmcnt(0)" ::: "memory");
    f32x4 pr[GEP];
#pragma unroll
    for (int u = 0; u < GEP; ++u) pr[u] = *reinterpret_cast<const f32x4*>(prow + 4 * u);
    if (tail) {
#pragma unroll
      for (int t = 0; t < 4; ++t) { const bool lv = (r + 4 * t) < rem;
#pragma unroll
        for (int k = 0; k < 4; ++k) Vc[t][k] = lv ? Vc[t][k] : 0u; } }
#pragma unroll
    for (int t = 0; t < 4; ++t) {
      float vf[8];
#pragma unroll
      for (int k = 0; k < 4; ++k) { vf[2 * k] = __builtin_bit_cast(float, Vc[t][k] << 16); vf[2 * k + 1] = __builtin_bit_cast(float, Vc[t][k] & 0xffff0000u); }
#pragma unroll
      for (int g = 0; g < GE; ++g) { const float pg_ = pr[(t * GEP + g) >> 2][(t * GEP + g) & 3];
#pragma unroll
        for (int k = 0; k < 8; ++k) acc[g * 8 + k] = fmaf(pg_, vf[k], acc[g * 8 + k]); } }
  }
#undef ISSUE
#pragma unroll
  for (int k = 0; k < GE * 8; ++k) acc[k] = xrow16_sum(acc[k]);
#pragma unroll
  for (int j = 0; j < HPL; ++j) { float t = l[j]; t += dpp<ROR4>(t); t += dpp<XOR8>(t); l[j] = xrow16_sum(t); }
  float* mrg = lds + LDS_MRG;
  if (r == 0) {
#pragma unroll
    for (int g = 0; g < GE; ++g) {
      float* rec = mrg + (wave * GEP + g) * MRG_REC;
#pragma unroll
      for (int k = 0; k < 8; k += 4) *reinterpret_cast<f32x4*>(rec + 4 + 8 * c + k) = f32x4{acc[g * 8 + k], acc[g * 8 + k + 1], acc[g * 8 + k + 2], acc[g * 8 + k + 3]};
      if (c == g / HPL) { rec[0] = m[g % HPL]; rec[1] = l[g % HPL]; } } }
  __syncthreads();
  float* pml = reinterpret_cast<float*>(a.ws + WS_PML);
  float* po = reinterpret_cast<float*>(a.ws + WS_PO);
#pragma unroll
  for (int jj = 0; jj < HPL; ++jj) {
    const int g = wave + 4 * jj, d0 = 2 * lane;
    if (g >= GE) continue;
    const size_t row = (size_t)b * HQ * SQ + kvh * GE + g;
    float M = -INFINITY;
#pragma unroll
    for (int w = 0; w < WAVES; ++w) M = fmaxf(M, mrg[(w * GEP + g) * MRG_REC]);
    float L = 0.f, o0 = 0.f, o1 = 0.f;
#pragma unroll
    for (int w = 0; w < WAVES; ++w) { const float* rec = mrg + (w * GEP + g) * MRG_REC; const float lw = rec[1];
      const float wt = (lw > 0.f) ? __builtin_amdgcn_exp2f(rec[0] - M) : 0.f;
      L = fmaf(wt, lw, L); o0 = fmaf(wt, rec[4 + d0], o0); o1 = fmaf(wt, rec[4 + d0 + 1], o1); }
    if constexpr (NSPLIT == 1) {
      const float inv = (L > 0.f) ? 1.f / L : 0.f;
      *reinterpret_cast<unsigned*>(a.out + row * D + d0) = cvtpk(o0 * inv, o1 * inv);
    } else {
      const size_t slot = row * NSPLIT + s;
      st_partial(reinterpret_cast<float2*>(po + slot * D + d0), make_float2(o0, o1));
      if (lane == 0) st_partial(reinterpret_cast<float2*>(pml + 2 * slot), make_float2(M, L));
    }
  }
  if constexpr (COMBINE == 1 && NSPLIT > 1) {
    asm volatile("s_waitcnt vmcnt(0)" ::: "memory");
    __syncthreads();
    if (tid == 0) {
      unsigned* cnt = reinterpret_cast<unsigned*>(a.ws + WS_CNT) + b * HKV + kvh;
      const unsigned old = __hip_atomic_fetch_add(cnt, 1u, __ATOMIC_RELAXED, __HIP_MEMORY_SCOPE_AGENT);
      const unsigned last = (old == (unsigned)(NSPLIT - 1)) ? 1u : 0u;
      if (last) __builtin_amdgcn_fence(__ATOMIC_ACQUIRE, "agent");
      reinterpret_cast<unsigned*>(lds + LDS_FLAG)[0] = last;
    }
    __syncthreads();
    if (reinterpret_cast<const unsigned*>(lds + LDS_FLAG)[0] == 0u) return;
#pragma unroll
    for (int jj = 0; jj < HPL; ++jj) { if (wave + 4 * jj >= GE) continue;
      const size_t row = (size_t)b * HQ * SQ + kvh * GE + wave + 4 * jj;
      merge_partials(pml + row * NSPLIT * 2, po + row * NSPLIT * D, NSPLIT, 2 * lane, a.out + row * D);
    }
  }
}

__global__ void __launch_bounds__(64) attn_decode_combine(const float* pml, const float* po, bf16* out) {
  const size_t row = (size_t)blockIdx.y * HQ * SQ + blockIdx.x;
  merge_partials(pml + row * NSPLIT * 2, po + row * NSPLIT * D, NSPLIT, 2 * (threadIdx.x & 63), out + row * D);
}

template <typename TIn>
static void attn_decode_launch(const TIn* q, const TIn* k, const TIn* v, const int* bt, const int* lens, bf16* out, void* ws, hipStream_t stream) {
  DecArgs<TIn> a{q, k, v, bt, lens, out, (unsigned char*)ws};
  if (COMBINE == 1 && NSPLIT > 1 && hipMemsetAsync((char*)ws + WS_CNT, 0, CNT_BYTES, stream) != hipSuccess) { fprintf(stderr, "kernel_launch: memset failed\n"); return; }
  hipLaunchKernelGGL(attn_decode_split<TIn>, dim3(NSPLIT, HKV, B), dim3(NTHR), 0, stream, a);
  if (COMBINE == 0 && NSPLIT > 1) hipLaunchKernelGGL(attn_decode_combine, dim3(HQ * SQ, B), dim3(64), 0, stream, (const float*)((char*)ws + WS_PML), (const float*)((char*)ws + WS_PO), out);
  const hipError_t le = hipPeekAtLastError();
  if (le != hipSuccess) fprintf(stderr, "kernel_launch: launch failed: %s\n", hipGetErrorName(le));
}

extern "C" void kernel_launch(void* const* d_in, const int* in_sizes, int n_in, void* d_out, int out_size, void* d_ws, size_t ws_size, hipStream_t stream) {
  static int checked = 0;
  if (checked == 0) {
    const int want_in = PAGED ? 5 : 3;
    const bool kv_ok = PAGED ? (in_sizes[1] % (PAGE * HKV * D) == 0 && in_sizes[1] >= B * NBPS * PAGE * HKV * D)
                             : (in_sizes[1] == B * HKV * CTX * D);
    const bool ok = n_in == want_in && in_sizes[0] == B * HQ * SQ * D && kv_ok && in_sizes[2] == in_sizes[1]
                 && (!PAGED || (in_sizes[3] == B * NBPS && in_sizes[4] == B)) && out_size == B * HQ * SQ * D && ws_size >= (NSPLIT > 1 ? WS_END : (size_t)0);
    if (!ok) { fprintf(stderr, "kernel_launch: unexpected problem shape (n_in %d, in0 %d, in1 %d, out %d, ws %zu; want B%d HQ%d HKV%d CTX%d paged%d)\n",
                       n_in, n_in > 0 ? in_sizes[0] : -1, n_in > 1 ? in_sizes[1] : -1, out_size, ws_size, B, HQ, HKV, CTX, PAGED); checked = -1; return; }
    checked = 1;
  }
  if (checked < 0) return;
  attn_decode_launch<bf16>((const bf16*)d_in[0], (const bf16*)d_in[1], (const bf16*)d_in[2], PAGED ? (const int*)d_in[3] : nullptr, PAGED ? (const int*)d_in[4] : nullptr,
                            (bf16*)d_out, d_ws, stream);
}
```

```cpp
#include <hip/hip_runtime.h>
#include <hip/hip_bf16.h>
#include <stdint.h>

#define P_TW 4
#define P_CT 2
#define P_DEPTH 3
#define P_AUX 2
#define P_OUTWT 1
#define P_PSLEEP 1
#define P_NWG 256

namespace {
constexpr int B = 2, H = 32, D = 128, PAGE = 16, NBPS = 512, SKV = NBPS * PAGE;
constexpr unsigned ROWB = H * D * 2;
constexpr unsigned PAGEB = PAGE * ROWB;
constexpr int TW = P_TW, CT = P_CT, DEPTH = P_DEPTH, NWG = P_NWG;
constexpr int NTHR = TW * 64;
constexpr int NCOL = 8;
constexpr int NS = NWG / NCOL;
constexpr int SEG = 63;
constexpr int SEGT = (SEG + 1) * CT;
constexpr int TSH = CT == 16 ? 0 : CT == 8 ? 1 : CT == 4 ? 2 : CT == 2 ? 3 : 4;
constexpr unsigned WS_LSE = 256, WS_SLOT = WS_LSE + NCOL * 4 * NS * 8, SLOT_B = 4096;
constexpr unsigned WS_BYTES = WS_SLOT + NWG * SLOT_B;
constexpr unsigned OUT_BYTES = B * H * D * 2;
constexpr float C2 = 0.088388347648318440f * 1.4426950408889634f;
constexpr float MINIT = -3.0e38f;
constexpr unsigned SPIN_CAP = 1u << 16;

typedef unsigned u32x4 __attribute__((ext_vector_type(4)));
typedef unsigned u32x2 __attribute__((ext_vector_type(2)));
typedef float f32x4 __attribute__((ext_vector_type(4)));
typedef float f32x2 __attribute__((ext_vector_type(2)));
typedef __bf16 bf16x2 __attribute__((ext_vector_type(2)));
typedef __attribute__((address_space(1))) unsigned gu32;

__device__ __forceinline__ float dot2(unsigned k, unsigned q, float acc) {
  return __builtin_amdgcn_fdot2_f32_bf16(__builtin_bit_cast(bf16x2, k), __builtin_bit_cast(bf16x2, q), acc, false);
}
template <int CTRL> __device__ __forceinline__ float dpp(float x) {
  return __builtin_bit_cast(float, __builtin_amdgcn_mov_dpp(__builtin_bit_cast(int, x), CTRL, 0xf, 0xf, true));
}
template <int N, int CTRL, int BIT> __device__ __forceinline__ void tstep(float* x, int c) {
  if constexpr (N >= 2) {
#pragma unroll
    for (int h = 0; h < N / 2; ++h) {
      const float lo = x[h], hi = x[h + N / 2];
      const float t1 = lo + dpp<CTRL>(lo), t2 = hi + dpp<CTRL>(hi);
      x[h] = (c & BIT) ? t2 : t1;
    }
  } else {
    x[0] += dpp<CTRL>(x[0]);
  }
}
__device__ __forceinline__ float xrow_max(float x) {
  auto s = __builtin_amdgcn_permlane16_swap(__float_as_uint(x), __float_as_uint(x), false, false);
  x = fmaxf(__uint_as_float(s[0]), __uint_as_float(s[1]));
  auto t = __builtin_amdgcn_permlane32_swap(__float_as_uint(x), __float_as_uint(x), false, false);
  return fmaxf(__uint_as_float(t[0]), __uint_as_float(t[1]));
}
__device__ __forceinline__ float xrow_sum(float x) {
  auto s = __builtin_amdgcn_permlane16_swap(__float_as_uint(x), __float_as_uint(x), false, false);
  x = __uint_as_float(s[0]) + __uint_as_float(s[1]);
  auto t = __builtin_amdgcn_permlane32_swap(__float_as_uint(x), __float_as_uint(x), false, false);
  return __uint_as_float(t[0]) + __uint_as_float(t[1]);
}
__device__ __forceinline__ unsigned pack_bf16(float lo, float hi) {
  bf16x2 v = {(__bf16)lo, (__bf16)hi};
  return __builtin_bit_cast(unsigned, v);
}
__device__ __forceinline__ int rfl(int x) { return __builtin_amdgcn_readfirstlane(x); }
__device__ __forceinline__ float f_(unsigned x) { return __builtin_bit_cast(float, x); }
__device__ __forceinline__ unsigned u_(float x) { return __builtin_bit_cast(unsigned, x); }
__device__ __forceinline__ int split_pt(int j, int n, int nc, float rn) {
  int f = (int)((float)(j * nc) * rn);
  f = f > nc ? nc : f;
  return j >= n ? nc : f;
}

struct Args {
  const unsigned short* q; const unsigned short* kc; const unsigned short* vc; const int* bt; const int* lens;
  unsigned short* out; unsigned char* ws; unsigned kv_bytes; unsigned pad;
};

__global__ void __launch_bounds__(NTHR, 2) attn_decode(Args a) {
  constexpr int LDS_ACC = 0, LDS_ML = TW * 512, LDS_BT = LDS_ML + TW * 8, LDS_CNT = LDS_BT + B * NBPS * TW, LDS_SC = LDS_CNT + 4,
                LDS_N = LDS_SC + TW * 4 * SEGT;
  __shared__ __attribute__((aligned(16))) float lds[LDS_N];
  const unsigned short* q = a.q; const unsigned short* kc = a.kc; const unsigned short* vc = a.vc;
  const int* bt = a.bt; const int* lens = a.lens; unsigned short* out = a.out; unsigned char* ws = a.ws;
  const unsigned kv_bytes = a.kv_bytes;
  asm volatile("" :: "s"(q), "s"(kc), "s"(vc), "s"(bt), "s"(lens), "s"(out), "s"(ws), "s"(kv_bytes));
  const int tid = threadIdx.x, lane = tid & 63, wave = rfl(tid >> 6);
  const int bid = blockIdx.x;
  const int col = bid / NS, s = bid % NS;
  const int hh = lane >> 4, c = lane & 15;
  u32x2 lenv = *reinterpret_cast<const u32x2*>(lens);
  u32x4 btp[4];
#pragma unroll
  for (int i = 0; i < 4; ++i) btp[i] = reinterpret_cast<const u32x4*>(bt)[i * 64 + lane];
  const unsigned voff = (unsigned)col * 1024u + (unsigned)lane * 16u;
  const u32x4 qa = *reinterpret_cast<const u32x4*>(reinterpret_cast<const unsigned char*>(q) + voff);
  const u32x4 qb = *reinterpret_cast<const u32x4*>(reinterpret_cast<const unsigned char*>(q) + ROWB + voff);
  const unsigned epoch = __hip_atomic_load((gu32*)ws, __ATOMIC_RELAXED, __HIP_MEMORY_SCOPE_AGENT);
  if (tid == 0) reinterpret_cast<unsigned*>(lds)[LDS_CNT] = 0u;
  asm volatile("" ::: "memory");
  __builtin_amdgcn_sched_barrier(0);
  asm volatile("" : "+v"(lenv));
  const int len0 = rfl((int)lenv[0]), len1 = rfl((int)lenv[1]);
#pragma unroll
  for (int i = 0; i < 4; ++i) *reinterpret_cast<u32x4*>(lds + LDS_BT + wave * (B * NBPS) + (i * 64 + lane) * 4) = btp[i];
  const int l0 = len0 <= 0 ? SKV : (len0 > SKV ? SKV : len0);
  const int l1 = len1 <= 0 ? SKV : (len1 > SKV ? SKV : len1);
  const int nc0 = (l0 + CT - 1) / CT, nc1 = (l1 + CT - 1) / CT;
  int t0_ = (int)((float)(NS * nc0) * __builtin_amdgcn_rcpf((float)(nc0 + nc1)) + 0.5f);
  t0_ = t0_ < 1 ? 1 : t0_; t0_ = t0_ > NS - 1 ? NS - 1 : t0_;
  const int n0 = rfl(t0_);
  const int b = s >= n0 ? 1 : 0;
  const int j = b ? s - n0 : s, n = b ? NS - n0 : n0, nc = b ? nc1 : nc0, len = b ? l1 : l0;
  const bool uni = (b ? len1 : len0) <= 0;
  const float rn = __builtin_amdgcn_rcpf((float)n);
  const int c0 = rfl(split_pt(j, n, nc, rn)), c1 = rfl(split_pt(j + 1, n, nc, rn));
  const int cw0 = c0 + wave;
  const int nw = c1 > cw0 ? (c1 - cw0 + TW - 1) / TW : 0;
  const __amdgpu_buffer_rsrc_t rk = __builtin_amdgcn_make_buffer_rsrc((void*)kc, 0, uni ? 0u : kv_bytes, 0x00020000);
  const __amdgpu_buffer_rsrc_t rv = __builtin_amdgcn_make_buffer_rsrc((void*)vc, 0, kv_bytes, 0x00020000);
  asm volatile("" ::: "memory");
  const int* lbt = reinterpret_cast<const int*>(lds + LDS_BT) + wave * (B * NBPS) + b * NBPS;
  float* sc = lds + LDS_SC + (wave * 4 + hh) * SEGT;

  u32x4 X[DEPTH][CT];
  float m = MINIT, l = 0.f, acc[8];
#pragma unroll
  for (int k = 0; k < 8; ++k) acc[k] = 0.f;

  int seg = 0;
  do {
    const int ns = min(nw - seg, SEG);
    const int cws = cw0 + seg * TW;
    unsigned offv[CT];
    {
      const int tok = (cws + lane * TW) * CT;
      const unsigned phys = (unsigned)lbt[min(tok >> 4, NBPS - 1)];
      const unsigned base = phys * PAGEB + (unsigned)(tok & 15) * ROWB;
#pragma unroll
      for (int t = 0; t < CT; ++t) offv[t] = (lane < ns && tok + t < len) ? base + t * ROWB : kv_bytes;
    }
#define ISSUE(d_, it_) do { \
      const bool isv_ = (it_) >= ns; \
      const int ci_ = min(isv_ ? (it_) - ns : (it_), 63); \
      const __amdgpu_buffer_rsrc_t r_ = isv_ ? rv : rk; \
      _Pragma("unroll") for (int t_ = 0; t_ < CT; ++t_) \
        X[d_][t_] = __builtin_amdgcn_raw_buffer_load_b128(r_, voff, (unsigned)__builtin_amdgcn_readlane((int)offv[t_], ci_), P_AUX); \
      __builtin_amdgcn_sched_barrier(0); \
    } while (0)
#pragma unroll
    for (int d = 0; d < DEPTH; ++d) ISSUE(d, d);
    if (seg == 0) {
      asm volatile("s_waitcnt lgkmcnt(0)" ::: "memory");
      __builtin_amdgcn_s_barrier();
      asm volatile("" ::: "memory");
    }
    u32x4 qv = b ? qb : qa;
    if (uni) qv = u32x4{0u, 0u, 0u, 0u};
    float mloc = m;
    for (int i0 = 0; i0 < 2 * ns; i0 += DEPTH) {
#pragma unroll
      for (int d = 0; d < DEPTH; ++d) {
        const int it = i0 + d;
        if (it < ns) {
          float x[CT];
#pragma unroll
          for (int t = 0; t < CT; ++t) {
            float y = dot2(X[d][t][0], qv[0], 0.f);
            y = dot2(X[d][t][1], qv[1], y); y = dot2(X[d][t][2], qv[2], y); x[t] = dot2(X[d][t][3], qv[3], y);
          }
          tstep<CT, 0x128, 8>(x, c); tstep<CT / 2, 0x141, 4>(x, c); tstep<CT / 4, 0x4E, 2>(x, c); tstep<CT / 8, 0xB1, 1>(x, c);
          const int rem = len - (cws + it * TW) * CT;
          const float sv = (c >> TSH) < rem ? x[0] * C2 : -INFINITY;
          mloc = fmaxf(mloc, sv);
          sc[it * CT + (c >> TSH)] = sv;
        } else if (it < 2 * ns) {
          if (it == ns) {
            float mn = mloc;
            mn = fmaxf(mn, dpp<0x128>(mn)); mn = fmaxf(mn, dpp<0x124>(mn)); mn = fmaxf(mn, dpp<0x122>(mn)); mn = fmaxf(mn, dpp<0x121>(mn));
            const float al = __builtin_amdgcn_exp2f(m - mn);
            m = mn; l *= al;
#pragma unroll
            for (int k = 0; k < 8; ++k) acc[k] *= al;
          }
          float p[CT];
#pragma unroll
          for (int t = 0; t < CT; ++t) p[t] = sc[(it - ns) * CT + t];
#pragma unroll
          for (int t = 0; t < CT; ++t) { p[t] = __builtin_amdgcn_exp2f(p[t] - m); l += p[t]; }
#pragma unroll
          for (int t = 0; t < CT; ++t) {
#pragma unroll
            for (int k = 0; k < 4; ++k) {
              acc[2 * k]     = fmaf(p[t], f_(X[d][t][k] << 16), acc[2 * k]);
              acc[2 * k + 1] = fmaf(p[t], f_(X[d][t][k] & 0xffff0000u), acc[2 * k + 1]);
            }
          }
        }
        __builtin_amdgcn_sched_barrier(0);
        ISSUE(d, it + DEPTH);
      }
    }
#undef ISSUE
    seg += SEG;
  } while (seg < nw);

  const unsigned tag = (unsigned)rfl((int)epoch) + 1u;
  const __amdgpu_buffer_rsrc_t rw = __builtin_amdgcn_make_buffer_rsrc((void*)ws, 0, WS_BYTES, 0x00020000);
  *reinterpret_cast<f32x4*>(lds + LDS_ACC + wave * 512 + lane * 8) = f32x4{acc[0], acc[1], acc[2], acc[3]};
  *reinterpret_cast<f32x4*>(lds + LDS_ACC + wave * 512 + lane * 8 + 4) = f32x4{acc[4], acc[5], acc[6], acc[7]};
  if (c == 0) *reinterpret_cast<f32x2*>(lds + LDS_ML + (hh * TW + wave) * 2) = f32x2{m, l};
  asm volatile("s_waitcnt lgkmcnt(0)" ::: "memory");
  unsigned old = 0;
  if (lane == 0) old = __hip_atomic_fetch_add(reinterpret_cast<unsigned*>(lds) + LDS_CNT, 1u, __ATOMIC_RELAXED, __HIP_MEMORY_SCOPE_WORKGROUP);
  const unsigned arrived = (unsigned)rfl((int)old);
  asm volatile("" ::: "memory");
  if (arrived == TW - 1) {
    float ml[2 * TW];
#pragma unroll
    for (int w = 0; w < TW; ++w) {
      const f32x2 x = *reinterpret_cast<const f32x2*>(lds + LDS_ML + (hh * TW + w) * 2);
      ml[2 * w] = x[0]; ml[2 * w + 1] = x[1];
    }
    float M = MINIT;
#pragma unroll
    for (int w = 0; w < TW; ++w) M = fmaxf(M, ml[2 * w]);
    float L = 0.f, o[8];
#pragma unroll
    for (int k = 0; k < 8; ++k) o[k] = 0.f;
#pragma unroll
    for (int w = 0; w < TW; ++w) {
      const float wt = __builtin_amdgcn_exp2f(ml[2 * w] - M);
      L = fmaf(wt, ml[2 * w + 1], L);
      const f32x4 x = *reinterpret_cast<const f32x4*>(lds + LDS_ACC + w * 512 + lane * 8);
      const f32x4 y = *reinterpret_cast<const f32x4*>(lds + LDS_ACC + w * 512 + lane * 8 + 4);
#pragma unroll
      for (int k = 0; k < 4; ++k) { o[k] = fmaf(wt, x[k], o[k]); o[4 + k] = fmaf(wt, y[k], o[4 + k]); }
    }
    const float inv = L > 0.f ? __builtin_amdgcn_rcpf(L) : 0.f;
    const float lse = L > 0.f ? M + __builtin_amdgcn_logf(L) : MINIT;
    const unsigned slot = WS_SLOT + (unsigned)bid * SLOT_B + (unsigned)lane * 16u;
#pragma unroll
    for (int jj = 0; jj < 4; ++jj)
      __builtin_amdgcn_raw_buffer_store_b128(u32x4{u_(o[2 * jj] * inv), tag, u_(o[2 * jj + 1] * inv), tag}, rw, slot + (unsigned)(jj * 1024), 0, 16);
    __builtin_amdgcn_raw_buffer_store_b64(u32x2{u_(lse), tag}, rw, c == 0 ? WS_LSE + (unsigned)(((col * 4 + hh) * NS + s) * 8) : WS_BYTES, 0, 16);
    constexpr int NB = 8;
    static_assert(4 * NB >= NS - 1 && NS == 32, "one slice per workgroup, one batch of partials");
    const int cb = s >> 4, hd = (s >> 2) & 3, jj = s & 3;
    const int cn = cb ? NS - n0 : n0, cs0 = cb ? n0 : 0;
    const unsigned sbase = WS_SLOT + (unsigned)(col * NS + cs0) * SLOT_B;
    const unsigned lbase = WS_LSE + (unsigned)((col * 4 + hd) * NS * 8);
    const unsigned doff = (unsigned)(jj * 1024 + hd * 256 + c * 16);
    float g0[NB], g1[NB], gs[NB];
    unsigned vo[NB], lo[NB];
#pragma unroll
    for (int k = 0; k < NB; ++k) {
      const bool want = 4 * k + hh < cn;
      vo[k] = want ? (unsigned)(4 * k + hh) * SLOT_B + doff : WS_BYTES;
      lo[k] = want ? (unsigned)(cs0 + 4 * k + hh) * 8u : WS_BYTES;
      g0[k] = 0.f; g1[k] = 0.f; gs[k] = MINIT;
    }
    unsigned km = (1u << ((cn + 3) >> 2)) - 1u;
    for (unsigned spins = 0; spins < SPIN_CAP; ++spins) {
      u32x4 x[NB]; u32x2 xl[NB];
#pragma unroll
      for (int k = 0; k < NB; ++k) {
        x[k] = u32x4{0u, 0u, 0u, 0u}; xl[k] = u32x2{0u, 0u};
        if ((km >> k) & 1u) {
          x[k] = __builtin_amdgcn_raw_buffer_load_b128(rw, vo[k], sbase, 16);
          xl[k] = __builtin_amdgcn_raw_buffer_load_b64(rw, lo[k], lbase, 16);
        }
      }
#pragma unroll
      for (int k = 0; k < NB; ++k) {
        if ((km >> k) & 1u) {
          const unsigned bad = (x[k][1] ^ tag) | (x[k][3] ^ tag) | (xl[k][1] ^ tag) | (vo[k] == WS_BYTES ? 1u : 0u);
          const bool ok = bad == 0u;
          g0[k] = ok ? f_(x[k][0]) : g0[k]; g1[k] = ok ? f_(x[k][2]) : g1[k]; gs[k] = ok ? f_(xl[k][0]) : gs[k];
          vo[k] = ok ? WS_BYTES : vo[k]; lo[k] = ok ? WS_BYTES : lo[k];
          if (!__any(vo[k] != WS_BYTES)) km &= ~(1u << k);
        }
      }
      if (km == 0u) break;
      if (P_PSLEEP) __builtin_amdgcn_s_sleep(P_PSLEEP);
    }
    float Mr = MINIT, Lr = 0.f, o0 = 0.f, o1 = 0.f;
#pragma unroll
    for (int k = 0; k < NB; ++k) Mr = fmaxf(Mr, gs[k]);
    const float Mx = xrow_max(Mr);
#pragma unroll
    for (int k = 0; k < NB; ++k) {
      const float wi = __builtin_amdgcn_exp2f(gs[k] - Mx);
      Lr += wi; o0 = fmaf(wi, g0[k], o0); o1 = fmaf(wi, g1[k], o1);
    }
    const float Lt = xrow_sum(Lr), t0 = xrow_sum(o0), t1 = xrow_sum(o1);
    const float iv = 1.0f / Lt;
    if (hh == 0)
      __hip_atomic_store((gu32*)(out + (cb * H + col * 4 + hd) * D + 8 * c + 2 * jj), pack_bf16(t0 * iv, t1 * iv), __ATOMIC_RELAXED, __HIP_MEMORY_SCOPE_AGENT);
  }
  if (bid == 0 && wave == 0) {
    for (unsigned spins = 0; spins < SPIN_CAP; ++spins) {
      bool ok = true;
#pragma unroll
      for (int i = 0; i < NWG / 64; ++i) {
        const u32x2 x = __builtin_amdgcn_raw_buffer_load_b64(rw, WS_LSE + (unsigned)((((i * 64 + lane) / NS) * 4 * NS + (lane % NS)) * 8), 0, 16);
        ok &= x[1] == tag;
      }
      if (__all(ok)) break;
      __builtin_amdgcn_s_sleep(8);
    }
    if (lane == 0) __hip_atomic_store((gu32*)ws, tag, __ATOMIC_RELAXED, __HIP_MEMORY_SCOPE_AGENT);
  }
}
}

extern "C" void kernel_launch(void* const* d_in, const int* in_sizes, int n_in, void* d_out, int out_size, void* d_ws, size_t ws_size,
                              hipStream_t stream) {
  const unsigned kv_bytes = (unsigned)in_sizes[1] * 2u;
  Args a{(const unsigned short*)d_in[0], (const unsigned short*)d_in[1], (const unsigned short*)d_in[2], (const int*)d_in[3],
         (const int*)d_in[4], (unsigned short*)d_out, (unsigned char*)d_ws, kv_bytes, 0u};
  hipLaunchKernelGGL(attn_decode, dim3(NWG), dim3(NTHR), 0, stream, a);
}
```

```cpp
#include <hip/hip_runtime.h>
#include <hip/hip_bf16.h>
#include <stdint.h>

#define P_TW 4
#define P_CT 2
#define P_DEPTH 3
#define P_AUX 2
#define P_PSLEEP 1
#define P_NWG 256

namespace {
constexpr int B = 2, H = 32, D = 128, PAGE = 16, NBPS = 512, SKV = NBPS * PAGE;
constexpr unsigned ROWB = H * D * 2;
constexpr unsigned PAGEB = PAGE * ROWB;
constexpr int TW = P_TW, CT = P_CT, DEPTH = P_DEPTH, NWG = P_NWG;
constexpr int NTHR = TW * 64;
constexpr int NCOL = 8;
constexpr int NS = NWG / NCOL;
constexpr int SEG = 63;
constexpr int SEGT = (SEG + 1) * CT;
constexpr int TSH = CT == 16 ? 0 : CT == 8 ? 1 : CT == 4 ? 2 : CT == 2 ? 3 : 4;
constexpr unsigned WS_LSE = 256, WS_SLOT = WS_LSE + NCOL * 4 * NS * 8, SLOT_B = 4096;
constexpr unsigned WS_BYTES = WS_SLOT + NWG * SLOT_B;
constexpr float C2 = 0.088388347648318440f * 1.4426950408889634f;
constexpr float MINIT = -3.0e38f;
constexpr unsigned SPIN_CAP = 1u << 16;

typedef unsigned u32x4 __attribute__((ext_vector_type(4)));
typedef unsigned u32x2 __attribute__((ext_vector_type(2)));
typedef float f32x4 __attribute__((ext_vector_type(4)));
typedef float f32x2 __attribute__((ext_vector_type(2)));
typedef __bf16 bf16x2 __attribute__((ext_vector_type(2)));
typedef __attribute__((address_space(1))) unsigned gu32;

__device__ __forceinline__ float dot2(unsigned k, unsigned q, float acc) {
  return __builtin_amdgcn_fdot2_f32_bf16(__builtin_bit_cast(bf16x2, k), __builtin_bit_cast(bf16x2, q), acc, false);
}
template <int CTRL> __device__ __forceinline__ float dpp(float x) {
  return __builtin_bit_cast(float, __builtin_amdgcn_mov_dpp(__builtin_bit_cast(int, x), CTRL, 0xf, 0xf, true));
}
template <int N, int CTRL, int BIT> __device__ __forceinline__ void tstep(float* x, int c) {
  if constexpr (N >= 2) {
#pragma unroll
    for (int h = 0; h < N / 2; ++h) {
      const float lo = x[h], hi = x[h + N / 2];
      const float t1 = lo + dpp<CTRL>(lo), t2 = hi + dpp<CTRL>(hi);
      x[h] = (c & BIT) ? t2 : t1;
    }
  } else {
    x[0] += dpp<CTRL>(x[0]);
  }
}
__device__ __forceinline__ float xrow_max(float x) {
  auto s = __builtin_amdgcn_permlane16_swap(__float_as_uint(x), __float_as_uint(x), false, false);
  x = fmaxf(__uint_as_float(s[0]), __uint_as_float(s[1]));
  auto t = __builtin_amdgcn_permlane32_swap(__float_as_uint(x), __float_as_uint(x), false, false);
  return fmaxf(__uint_as_float(t[0]), __uint_as_float(t[1]));
}
__device__ __forceinline__ float xrow_sum(float x) {
  auto s = __builtin_amdgcn_permlane16_swap(__float_as_uint(x), __float_as_uint(x), false, false);
  x = __uint_as_float(s[0]) + __uint_as_float(s[1]);
  auto t = __builtin_amdgcn_permlane32_swap(__float_as_uint(x), __float_as_uint(x), false, false);
  return __uint_as_float(t[0]) + __uint_as_float(t[1]);
}
__device__ __forceinline__ unsigned pack_bf16(float lo, float hi) {
  bf16x2 v = {(__bf16)lo, (__bf16)hi};
  return __builtin_bit_cast(unsigned, v);
}
__device__ __forceinline__ int rfl(int x) { return __builtin_amdgcn_readfirstlane(x); }
__device__ __forceinline__ float f_(unsigned x) { return __builtin_bit_cast(float, x); }
__device__ __forceinline__ unsigned u_(float x) { return __builtin_bit_cast(unsigned, x); }
__device__ __forceinline__ int split_pt(int j, int n, int nc, float rn) {
  int f = (int)((float)(j * nc) * rn);
  f = f > nc ? nc : f;
  return j >= n ? nc : f;
}

struct Args {
  const unsigned short* q; const unsigned short* kc; const unsigned short* vc; const int* bt; const int* lens;
  unsigned short* out; unsigned char* ws; unsigned kv_bytes; unsigned pad;
};

__global__ void __launch_bounds__(NTHR, 2) attn_decode(Args a) {
  constexpr int LDS_ACC = 0, LDS_ML = TW * 512, LDS_BT = LDS_ML + TW * 8, LDS_CNT = LDS_BT + B * NBPS * TW, LDS_SC = LDS_CNT + 4,
                LDS_N = LDS_SC + TW * 4 * SEGT;
  __shared__ __attribute__((aligned(16))) float lds[LDS_N];
  const unsigned short* q = a.q; const unsigned short* kc = a.kc; const unsigned short* vc = a.vc;
  const int* bt = a.bt; const int* lens = a.lens; unsigned short* out = a.out; unsigned char* ws = a.ws;
  const unsigned kv_bytes = a.kv_bytes;
  asm volatile("" :: "s"(q), "s"(kc), "s"(vc), "s"(bt), "s"(lens), "s"(out), "s"(ws), "s"(kv_bytes));
  const int tid = threadIdx.x, lane = tid & 63, wave = rfl(tid >> 6);
  const int bid = blockIdx.x;
  const int col = bid / NS, s = bid % NS;
  const int hh = lane >> 4, c = lane & 15;
  u32x2 lenv = *reinterpret_cast<const u32x2*>(lens);
  u32x4 btp[4];
#pragma unroll
  for (int i = 0; i < 4; ++i) btp[i] = reinterpret_cast<const u32x4*>(bt)[i * 64 + lane];
  const unsigned voff = (unsigned)col * 1024u + (unsigned)lane * 16u;
  const u32x4 qa = *reinterpret_cast<const u32x4*>(reinterpret_cast<const unsigned char*>(q) + voff);
  const u32x4 qb = *reinterpret_cast<const u32x4*>(reinterpret_cast<const unsigned char*>(q) + ROWB + voff);
  const unsigned epoch = __hip_atomic_load((gu32*)ws, __ATOMIC_RELAXED, __HIP_MEMORY_SCOPE_AGENT);
  if (tid == 0) reinterpret_cast<unsigned*>(lds)[LDS_CNT] = 0u;
  asm volatile("" ::: "memory");
  __builtin_amdgcn_sched_barrier(0);
  asm volatile("" : "+v"(lenv));
  const int len0 = rfl((int)lenv[0]), len1 = rfl((int)lenv[1]);
#pragma unroll
  for (int i = 0; i < 4; ++i) *reinterpret_cast<u32x4*>(lds + LDS_BT + wave * (B * NBPS) + (i * 64 + lane) * 4) = btp[i];
  const int l0 = len0 <= 0 ? SKV : (len0 > SKV ? SKV : len0);
  const int l1 = len1 <= 0 ? SKV : (len1 > SKV ? SKV : len1);
  const int nc0 = (l0 + CT - 1) / CT, nc1 = (l1 + CT - 1) / CT;
  int t0_ = (int)((float)(NS * nc0) * __builtin_amdgcn_rcpf((float)(nc0 + nc1)) + 0.5f);
  t0_ = t0_ < 1 ? 1 : t0_; t0_ = t0_ > NS - 1 ? NS - 1 : t0_;
  const int n0 = rfl(t0_);
  const int b = s >= n0 ? 1 : 0;
  const int j = b ? s - n0 : s, n = b ? NS - n0 : n0, nc = b ? nc1 : nc0, len = b ? l1 : l0;
  const bool uni = (b ? len1 : len0) <= 0;
  const float rn = __builtin_amdgcn_rcpf((float)n);
  const int c0 = rfl(split_pt(j, n, nc, rn)), c1 = rfl(split_pt(j + 1, n, nc, rn));
  const int cw0 = c0 + wave;
  const int nw = c1 > cw0 ? (c1 - cw0 + TW - 1) / TW : 0;
  const __amdgpu_buffer_rsrc_t rk = __builtin_amdgcn_make_buffer_rsrc((void*)kc, 0, uni ? 0u : kv_bytes, 0x00020000);
  const __amdgpu_buffer_rsrc_t rv = __builtin_amdgcn_make_buffer_rsrc((void*)vc, 0, kv_bytes, 0x00020000);
  asm volatile("" ::: "memory");
  const int* lbt = reinterpret_cast<const int*>(lds + LDS_BT) + wave * (B * NBPS) + b * NBPS;
  float* sc = lds + LDS_SC + (wave * 4 + hh) * SEGT;

  u32x4 X[DEPTH][CT];
  float m = MINIT, l = 0.f, acc[8];
#pragma unroll
  for (int k = 0; k < 8; ++k) acc[k] = 0.f;

  int seg = 0;
  do {
    const int ns = min(nw - seg, SEG);
    const int cws = cw0 + seg * TW;
    unsigned offv[CT];
    {
      const int tok = (cws + lane * TW) * CT;
      const unsigned phys = (unsigned)lbt[min(tok >> 4, NBPS - 1)];
      const unsigned base = phys * PAGEB + (unsigned)(tok & 15) * ROWB;
#pragma unroll
      for (int t = 0; t < CT; ++t) offv[t] = (lane < ns && tok + t < len) ? base + t * ROWB : kv_bytes;
    }
#define ISSUE(d_, it_) do { \
      const bool isv_ = (it_) >= ns; \
      const int ci_ = min(isv_ ? (it_) - ns : (it_), 63); \
      const __amdgpu_buffer_rsrc_t r_ = isv_ ? rv : rk; \
      _Pragma("unroll") for (int t_ = 0; t_ < CT; ++t_) \
        X[d_][t_] = __builtin_amdgcn_raw_buffer_load_b128(r_, voff, (unsigned)__builtin_amdgcn_readlane((int)offv[t_], ci_), P_AUX); \
      __builtin_amdgcn_sched_barrier(0); \
    } while (0)
#pragma unroll
    for (int d = 0; d < DEPTH; ++d) ISSUE(d, d);
    if (seg == 0) {
      asm volatile("s_waitcnt lgkmcnt(0)" ::: "memory");
      __builtin_amdgcn_s_barrier();
      asm volatile("" ::: "memory");
    }
    u32x4 qv = b ? qb : qa;
    if (uni) qv = u32x4{0u, 0u, 0u, 0u};
    float mloc = m;
    for (int i0 = 0; i0 < 2 * ns; i0 += DEPTH) {
#pragma unroll
      for (int d = 0; d < DEPTH; ++d) {
        const int it = i0 + d;
        if (it < ns) {
          float x[CT];
#pragma unroll
          for (int t = 0; t < CT; ++t) {
            float y = dot2(X[d][t][0], qv[0], 0.f);
            y = dot2(X[d][t][1], qv[1], y); y = dot2(X[d][t][2], qv[2], y); x[t] = dot2(X[d][t][3], qv[3], y);
          }
          tstep<CT, 0x128, 8>(x, c); tstep<CT / 2, 0x141, 4>(x, c); tstep<CT / 4, 0x4E, 2>(x, c); tstep<CT / 8, 0xB1, 1>(x, c);
          const int rem = len - (cws + it * TW) * CT;
          const float sv = (c >> TSH) < rem ? x[0] * C2 : -INFINITY;
          mloc = fmaxf(mloc, sv);
          sc[it * CT + (c >> TSH)] = sv;
        } else if (it < 2 * ns) {
          if (it == ns) {
            float mn = mloc;
            mn = fmaxf(mn, dpp<0x128>(mn)); mn = fmaxf(mn, dpp<0x124>(mn)); mn = fmaxf(mn, dpp<0x122>(mn)); mn = fmaxf(mn, dpp<0x121>(mn));
            const float al = __builtin_amdgcn_exp2f(m - mn);
            m = mn; l *= al;
#pragma unroll
            for (int k = 0; k < 8; ++k) acc[k] *= al;
          }
          float p[CT];
#pragma unroll
          for (int t = 0; t < CT; ++t) p[t] = sc[(it - ns) * CT + t];
#pragma unroll
          for (int t = 0; t < CT; ++t) { p[t] = __builtin_amdgcn_exp2f(p[t] - m); l += p[t]; }
#pragma unroll
          for (int t = 0; t < CT; ++t) {
#pragma unroll
            for (int k = 0; k < 4; ++k) {
              acc[2 * k]     = fmaf(p[t], f_(X[d][t][k] << 16), acc[2 * k]);
              acc[2 * k + 1] = fmaf(p[t], f_(X[d][t][k] & 0xffff0000u), acc[2 * k + 1]);
            }
          }
        }
        __builtin_amdgcn_sched_barrier(0);
        ISSUE(d, it + DEPTH);
      }
    }
#undef ISSUE
    seg += SEG;
  } while (seg < nw);

  const unsigned tag = (unsigned)rfl((int)epoch) + 1u;
  const __amdgpu_buffer_rsrc_t rw = __builtin_amdgcn_make_buffer_rsrc((void*)ws, 0, WS_BYTES, 0x00020000);
  *reinterpret_cast<f32x4*>(lds + LDS_ACC + wave * 512 + lane * 8) = f32x4{acc[0], acc[1], acc[2], acc[3]};
  *reinterpret_cast<f32x4*>(lds + LDS_ACC + wave * 512 + lane * 8 + 4) = f32x4{acc[4], acc[5], acc[6], acc[7]};
  if (c == 0) *reinterpret_cast<f32x2*>(lds + LDS_ML + (hh * TW + wave) * 2) = f32x2{m, l};
  asm volatile("s_waitcnt lgkmcnt(0)" ::: "memory");
  unsigned old = 0;
  if (lane == 0) old = __hip_atomic_fetch_add(reinterpret_cast<unsigned*>(lds) + LDS_CNT, 1u, __ATOMIC_RELAXED, __HIP_MEMORY_SCOPE_WORKGROUP);
  const unsigned arrived = (unsigned)rfl((int)old);
  asm volatile("" ::: "memory");
  if (arrived == TW - 1) {
    float ml[2 * TW];
#pragma unroll
    for (int w = 0; w < TW; ++w) {
      const f32x2 x = *reinterpret_cast<const f32x2*>(lds + LDS_ML + (hh * TW + w) * 2);
      ml[2 * w] = x[0]; ml[2 * w + 1] = x[1];
    }
    float M = MINIT;
#pragma unroll
    for (int w = 0; w < TW; ++w) M = fmaxf(M, ml[2 * w]);
    float L = 0.f, o[8];
#pragma unroll
    for (int k = 0; k < 8; ++k) o[k] = 0.f;
#pragma unroll
    for (int w = 0; w < TW; ++w) {
      const float wt = __builtin_amdgcn_exp2f(ml[2 * w] - M);
      L = fmaf(wt, ml[2 * w + 1], L);
#pragma unroll
      for (int g = 0; g < 4; ++g) {
        const f32x2 x = *reinterpret_cast<const f32x2*>(lds + LDS_ACC + w * 512 + hh * 128 + g * 32 + 2 * c);
        o[2 * g] = fmaf(wt, x[0], o[2 * g]); o[2 * g + 1] = fmaf(wt, x[1], o[2 * g + 1]);
      }
    }
    const float inv = L > 0.f ? __builtin_amdgcn_rcpf(L) : 0.f;
    const float lse = L > 0.f ? M + __builtin_amdgcn_logf(L) : MINIT;
    const unsigned slot = WS_SLOT + (unsigned)bid * SLOT_B + (unsigned)lane * 16u;
#pragma unroll
    for (int jj = 0; jj < 4; ++jj)
      __builtin_amdgcn_raw_buffer_store_b128(u32x4{u_(o[2 * jj] * inv), tag, u_(o[2 * jj + 1] * inv), tag}, rw, slot + (unsigned)(jj * 1024), 0, 16);
    __builtin_amdgcn_raw_buffer_store_b64(u32x2{u_(lse), tag}, rw, c == 0 ? WS_LSE + (unsigned)(((col * 4 + hh) * NS + s) * 8) : WS_BYTES, 0, 16);
    constexpr int NB = 8;
    static_assert(4 * NB >= NS - 1 && NS == 32, "one slice per workgroup, one batch of partials");
    const int cb = s >> 4, hd = (s >> 2) & 3, jj = s & 3;
    const int cn = cb ? NS - n0 : n0, cs0 = cb ? n0 : 0;
    const unsigned sbase = WS_SLOT + (unsigned)(col * NS + cs0) * SLOT_B;
    const unsigned lbase = WS_LSE + (unsigned)((col * 4 + hd) * NS * 8);
    const unsigned doff = (unsigned)(jj * 1024 + hd * 256 + c * 16);
    float g0[NB], g1[NB], gs[NB];
    unsigned vo[NB], lo[NB];
#pragma unroll
    for (int k = 0; k < NB; ++k) {
      const bool want = 4 * k + hh < cn;
      vo[k] = want ? (unsigned)(4 * k + hh) * SLOT_B + doff : WS_BYTES;
      lo[k] = want ? (unsigned)(cs0 + 4 * k + hh) * 8u : WS_BYTES;
      g0[k] = 0.f; g1[k] = 0.f; gs[k] = MINIT;
    }
    unsigned km = (1u << ((cn + 3) >> 2)) - 1u;
    for (unsigned spins = 0; spins < SPIN_CAP; ++spins) {
      asm volatile("" ::: "memory");
      u32x4 x[NB]; u32x2 xl[NB];
#pragma unroll
      for (int k = 0; k < NB; ++k) {
        x[k] = u32x4{0u, 0u, 0u, 0u}; xl[k] = u32x2{0u, 0u};
        if ((km >> k) & 1u) {
          x[k] = __builtin_amdgcn_raw_buffer_load_b128(rw, vo[k], sbase, 16);
          xl[k] = __builtin_amdgcn_raw_buffer_load_b64(rw, lo[k], lbase, 16);
        }
      }
#pragma unroll
      for (int k = 0; k < NB; ++k) {
        if ((km >> k) & 1u) {
          const unsigned bad = (x[k][1] ^ tag) | (x[k][3] ^ tag) | (xl[k][1] ^ tag) | (vo[k] == WS_BYTES ? 1u : 0u);
          const bool ok = bad == 0u;
          g0[k] = ok ? f_(x[k][0]) : g0[k]; g1[k] = ok ? f_(x[k][2]) : g1[k]; gs[k] = ok ? f_(xl[k][0]) : gs[k];
          vo[k] = ok ? WS_BYTES : vo[k]; lo[k] = ok ? WS_BYTES : lo[k];
          if (!__any(vo[k] != WS_BYTES)) km &= ~(1u << k);
        }
      }
      if (km == 0u) break;
      if (P_PSLEEP) __builtin_amdgcn_s_sleep(P_PSLEEP);
    }
    float Mr = MINIT, Lr = 0.f, o0 = 0.f, o1 = 0.f;
#pragma unroll
    for (int k = 0; k < NB; ++k) Mr = fmaxf(Mr, gs[k]);
    const float Mx = xrow_max(Mr);
#pragma unroll
    for (int k = 0; k < NB; ++k) {
      const float wi = __builtin_amdgcn_exp2f(gs[k] - Mx);
      Lr += wi; o0 = fmaf(wi, g0[k], o0); o1 = fmaf(wi, g1[k], o1);
    }
    const float Lt = xrow_sum(Lr), t0 = xrow_sum(o0), t1 = xrow_sum(o1);
    const float iv = 1.0f / Lt;
    if (hh == 0)
      __hip_atomic_store((gu32*)(out + (cb * H + col * 4 + hd) * D + 32 * jj + 2 * c), pack_bf16(t0 * iv, t1 * iv), __ATOMIC_RELAXED, __HIP_MEMORY_SCOPE_AGENT);
  }
  if (bid == 0 && wave == 0) {
    for (unsigned spins = 0; spins < SPIN_CAP; ++spins) {
      asm volatile("" ::: "memory");
      bool ok = true;
#pragma unroll
      for (int i = 0; i < NWG / 64; ++i) {
        const u32x2 x = __builtin_amdgcn_raw_buffer_load_b64(rw, WS_LSE + (unsigned)((((i * 64 + lane) / NS) * 4 * NS + (lane % NS)) * 8), 0, 16);
        ok &= x[1] == tag;
      }
      if (__all(ok)) break;
      __builtin_amdgcn_s_sleep(8);
    }
    if (lane == 0) __hip_atomic_store((gu32*)ws, tag, __ATOMIC_RELAXED, __HIP_MEMORY_SCOPE_AGENT);
  }
}
}

extern "C" void kernel_launch(void* const* d_in, const int* in_sizes, int n_in, void* d_out, int out_size, void* d_ws, size_t ws_size,
                              hipStream_t stream) {
  const unsigned kv_bytes = (unsigned)in_sizes[1] * 2u;
  Args a{(const unsigned short*)d_in[0], (const unsigned short*)d_in[1], (const unsigned short*)d_in[2], (const int*)d_in[3],
         (const int*)d_in[4], (unsigned short*)d_out, (unsigned char*)d_ws, kv_bytes, 0u};
  hipLaunchKernelGGL(attn_decode, dim3(NWG), dim3(NTHR), 0, stream, a);
}
```

```cpp
#include <hip/hip_runtime.h>
#include <hip/hip_bf16.h>
#include <stdint.h>

#define P_TW 4
#define P_CT 2
#define P_DEPTH 3
#define P_AUX 2
#define P_RV P_DEPTH
#define P_PSLEEP 1
#define P_NWG 256

namespace {
constexpr int B = 2, H = 32, D = 128, PAGE = 16, NBPS = 512, SKV = NBPS * PAGE;
constexpr unsigned ROWB = H * D * 2;
constexpr unsigned PAGEB = PAGE * ROWB;
constexpr int TW = P_TW, CT = P_CT, DEPTH = P_DEPTH, NWG = P_NWG;
constexpr int NTHR = TW * 64;
constexpr int NCOL = 8;
constexpr int NS = NWG / NCOL;
constexpr int SEG = 63;
constexpr int SEGT = (SEG + 1) * CT;
constexpr int TSH = CT == 16 ? 0 : CT == 8 ? 1 : CT == 4 ? 2 : CT == 2 ? 3 : 4;
constexpr unsigned WS_LSE = 256, WS_SLOT = WS_LSE + NCOL * 4 * NS * 8, SLOT_B = 4096;
constexpr unsigned WS_BYTES = WS_SLOT + NWG * SLOT_B;
constexpr float C2 = 0.088388347648318440f * 1.4426950408889634f;
constexpr float MINIT = -3.0e38f;
constexpr unsigned SPIN_CAP = 1u << 16;

typedef unsigned u32x4 __attribute__((ext_vector_type(4)));
typedef unsigned u32x2 __attribute__((ext_vector_type(2)));
typedef float f32x4 __attribute__((ext_vector_type(4)));
typedef float f32x2 __attribute__((ext_vector_type(2)));
typedef __bf16 bf16x2 __attribute__((ext_vector_type(2)));
typedef __attribute__((address_space(1))) unsigned gu32;

__device__ __forceinline__ float dot2(unsigned k, unsigned q, float acc) {
  return __builtin_amdgcn_fdot2_f32_bf16(__builtin_bit_cast(bf16x2, k), __builtin_bit_cast(bf16x2, q), acc, false);
}
template <int CTRL> __device__ __forceinline__ float dpp(float x) {
  return __builtin_bit_cast(float, __builtin_amdgcn_mov_dpp(__builtin_bit_cast(int, x), CTRL, 0xf, 0xf, true));
}
template <int N, int CTRL, int BIT> __device__ __forceinline__ void tstep(float* x, int c) {
  if constexpr (N >= 2) {
#pragma unroll
    for (int h = 0; h < N / 2; ++h) {
      const float lo = x[h], hi = x[h + N / 2];
      const float t1 = lo + dpp<CTRL>(lo), t2 = hi + dpp<CTRL>(hi);
      x[h] = (c & BIT) ? t2 : t1;
    }
  } else {
    x[0] += dpp<CTRL>(x[0]);
  }
}
__device__ __forceinline__ float xrow_max(float x) {
  auto s = __builtin_amdgcn_permlane16_swap(__float_as_uint(x), __float_as_uint(x), false, false);
  x = fmaxf(__uint_as_float(s[0]), __uint_as_float(s[1]));
  auto t = __builtin_amdgcn_permlane32_swap(__float_as_uint(x), __float_as_uint(x), false, false);
  return fmaxf(__uint_as_float(t[0]), __uint_as_float(t[1]));
}
__device__ __forceinline__ float xrow_sum(float x) {
  auto s = __builtin_amdgcn_permlane16_swap(__float_as_uint(x), __float_as_uint(x), false, false);
  x = __uint_as_float(s[0]) + __uint_as_float(s[1]);
  auto t = __builtin_amdgcn_permlane32_swap(__float_as_uint(x), __float_as_uint(x), false, false);
  return __uint_as_float(t[0]) + __uint_as_float(t[1]);
}
__device__ __forceinline__ unsigned pack_bf16(float lo, float hi) {
  bf16x2 v = {(__bf16)lo, (__bf16)hi};
  return __builtin_bit_cast(unsigned, v);
}
__device__ __forceinline__ int rfl(int x) { return __builtin_amdgcn_readfirstlane(x); }
__device__ __forceinline__ float f_(unsigned x) { return __builtin_bit_cast(float, x); }
__device__ __forceinline__ unsigned u_(float x) { return __builtin_bit_cast(unsigned, x); }
__device__ __forceinline__ int split_pt(int j, int n, int nc, float rn) {
  int f = (int)((float)(j * nc) * rn);
  f = f > nc ? nc : f;
  return j >= n ? nc : f;
}

struct Args {
  const unsigned short* q; const unsigned short* kc; const unsigned short* vc; const int* bt; const int* lens;
  unsigned short* out; unsigned char* ws; unsigned kv_bytes; unsigned pad;
};

__global__ void __launch_bounds__(NTHR, 2) attn_decode(Args a) {
  constexpr int LDS_ACC = 0, LDS_ML = TW * 512, LDS_BT = LDS_ML + TW * 8, LDS_CNT = LDS_BT + B * NBPS * TW, LDS_SC = LDS_CNT + 4,
                LDS_N = LDS_SC + TW * 4 * SEGT;
  __shared__ __attribute__((aligned(16))) float lds[LDS_N];
  const unsigned short* q = a.q; const unsigned short* kc = a.kc; const unsigned short* vc = a.vc;
  const int* bt = a.bt; const int* lens = a.lens; unsigned short* out = a.out; unsigned char* ws = a.ws;
  const unsigned kv_bytes = a.kv_bytes;
  asm volatile("" :: "s"(q), "s"(kc), "s"(vc), "s"(bt), "s"(lens), "s"(out), "s"(ws), "s"(kv_bytes));
  const int tid = threadIdx.x, lane = tid & 63, wave = rfl(tid >> 6);
  const int bid = blockIdx.x;
  const int col = bid / NS, s = bid % NS;
  const int hh = lane >> 4, c = lane & 15;
  u32x2 lenv = *reinterpret_cast<const u32x2*>(lens);
  u32x4 btp[4];
#pragma unroll
  for (int i = 0; i < 4; ++i) btp[i] = reinterpret_cast<const u32x4*>(bt)[i * 64 + lane];
  const unsigned voff = (unsigned)col * 1024u + (unsigned)lane * 16u;
  const u32x4 qa = *reinterpret_cast<const u32x4*>(reinterpret_cast<const unsigned char*>(q) + voff);
  const u32x4 qb = *reinterpret_cast<const u32x4*>(reinterpret_cast<const unsigned char*>(q) + ROWB + voff);
  const unsigned epoch = __hip_atomic_load((gu32*)ws, __ATOMIC_RELAXED, __HIP_MEMORY_SCOPE_AGENT);
  if (tid == 0) reinterpret_cast<unsigned*>(lds)[LDS_CNT] = 0u;
  asm volatile("" ::: "memory");
  __builtin_amdgcn_sched_barrier(0);
  asm volatile("" : "+v"(lenv));
  const int len0 = rfl((int)lenv[0]), len1 = rfl((int)lenv[1]);
#pragma unroll
  for (int i = 0; i < 4; ++i) *reinterpret_cast<u32x4*>(lds + LDS_BT + wave * (B * NBPS) + (i * 64 + lane) * 4) = btp[i];
  const int l0 = len0 <= 0 ? SKV : (len0 > SKV ? SKV : len0);
  const int l1 = len1 <= 0 ? SKV : (len1 > SKV ? SKV : len1);
  const int nc0 = (l0 + CT - 1) / CT, nc1 = (l1 + CT - 1) / CT;
  int t0_ = (int)((float)(NS * nc0) * __builtin_amdgcn_rcpf((float)(nc0 + nc1)) + 0.5f);
  t0_ = t0_ < 1 ? 1 : t0_; t0_ = t0_ > NS - 1 ? NS - 1 : t0_;
  const int n0 = rfl(t0_);
  const int b = s >= n0 ? 1 : 0;
  const int j = b ? s - n0 : s, n = b ? NS - n0 : n0, nc = b ? nc1 : nc0, len = b ? l1 : l0;
  const bool uni = (b ? len1 : len0) <= 0;
  const float rn = __builtin_amdgcn_rcpf((float)n);
  const int c0 = rfl(split_pt(j, n, nc, rn)), c1 = rfl(split_pt(j + 1, n, nc, rn));
  const int cw0 = c0 + wave;
  const int nw = c1 > cw0 ? (c1 - cw0 + TW - 1) / TW : 0;
  const __amdgpu_buffer_rsrc_t rk = __builtin_amdgcn_make_buffer_rsrc((void*)kc, 0, uni ? 0u : kv_bytes, 0x00020000);
  const __amdgpu_buffer_rsrc_t rv = __builtin_amdgcn_make_buffer_rsrc((void*)vc, 0, kv_bytes, 0x00020000);
  asm volatile("" ::: "memory");
  const int* lbt = reinterpret_cast<const int*>(lds + LDS_BT) + wave * (B * NBPS) + b * NBPS;
  float* sc = lds + LDS_SC + (wave * 4 + hh) * SEGT;

  constexpr int RK = DEPTH, RV = P_RV;
  static_assert(RV >= RK, "V ring at least as deep as the K ring");
  u32x4 X[RV][CT];
  float m = MINIT, l = 0.f, acc[8];
#pragma unroll
  for (int k = 0; k < 8; ++k) acc[k] = 0.f;

  int seg = 0;
  do {
    const int ns = min(nw - seg, SEG);
    const int cws = cw0 + seg * TW;
    unsigned offv[CT];
    {
      const int tok = (cws + lane * TW) * CT;
      const unsigned phys = (unsigned)lbt[min(tok >> 4, NBPS - 1)];
      const unsigned base = phys * PAGEB + (unsigned)(tok & 15) * ROWB;
#pragma unroll
      for (int t = 0; t < CT; ++t) offv[t] = (lane < ns && tok + t < len) ? base + t * ROWB : kv_bytes;
    }
#define LOADS(d_, r_, ci_) do { \
      _Pragma("unroll") for (int t_ = 0; t_ < CT; ++t_) \
        X[d_][t_] = __builtin_amdgcn_raw_buffer_load_b128(r_, voff, (unsigned)__builtin_amdgcn_readlane((int)offv[t_], (ci_)), P_AUX); \
      __builtin_amdgcn_sched_barrier(0); \
    } while (0)
    const int pad = (RK - ns % RK) % RK;
#define ISSUE(d_, it_) do { \
      const bool isv_ = (it_) >= ns; \
      const int ci_ = (it_) < 0 ? 63 : min(isv_ ? (it_) - ns : (it_), 63); \
      const __amdgpu_buffer_rsrc_t r_ = isv_ ? rv : rk; \
      LOADS(d_, r_, ci_); \
    } while (0)
#pragma unroll
    for (int d = 0; d < RK; ++d) ISSUE(d, d - pad);
    if (seg == 0) {
      asm volatile("s_waitcnt lgkmcnt(0)" ::: "memory");
      __builtin_amdgcn_s_barrier();
      asm volatile("" ::: "memory");
    }
    u32x4 qv = b ? qb : qa;
    if (uni) qv = u32x4{0u, 0u, 0u, 0u};
    float mloc = m;
    for (int i0 = -pad; i0 < ns; i0 += RK) {
#pragma unroll
      for (int d = 0; d < RK; ++d) {
        const int it = i0 + d;
        if (it >= 0) {
          float x[CT];
#pragma unroll
          for (int t = 0; t < CT; ++t) {
            float y = dot2(X[d][t][0], qv[0], 0.f);
            y = dot2(X[d][t][1], qv[1], y); y = dot2(X[d][t][2], qv[2], y); x[t] = dot2(X[d][t][3], qv[3], y);
          }
          tstep<CT, 0x128, 8>(x, c); tstep<CT / 2, 0x141, 4>(x, c); tstep<CT / 4, 0x4E, 2>(x, c); tstep<CT / 8, 0xB1, 1>(x, c);
          const int rem = len - (cws + it * TW) * CT;
          const float sv = (c >> TSH) < rem ? x[0] * C2 : -INFINITY;
          mloc = fmaxf(mloc, sv);
          sc[it * CT + (c >> TSH)] = sv;
        }
        __builtin_amdgcn_sched_barrier(0);
        ISSUE(d, it + RK);
      }
    }
#pragma unroll
    for (int d = RK; d < RV; ++d) LOADS(d, rv, min(d, 63));
    asm volatile("" ::: "memory");
    {
      float mn = mloc;
      mn = fmaxf(mn, dpp<0x128>(mn)); mn = fmaxf(mn, dpp<0x124>(mn)); mn = fmaxf(mn, dpp<0x122>(mn)); mn = fmaxf(mn, dpp<0x121>(mn));
      const float al = __builtin_amdgcn_exp2f(m - mn);
      m = mn; l *= al;
#pragma unroll
      for (int k = 0; k < 8; ++k) acc[k] *= al;
    }
    for (int i0 = 0; i0 < ns; i0 += RV) {
#pragma unroll
      for (int d = 0; d < RV; ++d) {
        const int it = i0 + d;
        if (it < ns) {
          float p[CT];
#pragma unroll
          for (int t = 0; t < CT; ++t) p[t] = sc[it * CT + t];
#pragma unroll
          for (int t = 0; t < CT; ++t) { p[t] = __builtin_amdgcn_exp2f(p[t] - m); l += p[t]; }
#pragma unroll
          for (int t = 0; t < CT; ++t) {
#pragma unroll
            for (int k = 0; k < 4; ++k) {
              acc[2 * k]     = fmaf(p[t], f_(X[d][t][k] << 16), acc[2 * k]);
              acc[2 * k + 1] = fmaf(p[t], f_(X[d][t][k] & 0xffff0000u), acc[2 * k + 1]);
            }
          }
        }
        __builtin_amdgcn_sched_barrier(0);
        LOADS(d, rv, min(it + RV, 63));
      }
    }
#undef LOADS
#undef ISSUE
    seg += SEG;
  } while (seg < nw);

  const unsigned tag = (unsigned)rfl((int)epoch) + 1u;
  const __amdgpu_buffer_rsrc_t rw = __builtin_amdgcn_make_buffer_rsrc((void*)ws, 0, WS_BYTES, 0x00020000);
  *reinterpret_cast<f32x4*>(lds + LDS_ACC + wave * 512 + lane * 8) = f32x4{acc[0], acc[1], acc[2], acc[3]};
  *reinterpret_cast<f32x4*>(lds + LDS_ACC + wave * 512 + lane * 8 + 4) = f32x4{acc[4], acc[5], acc[6], acc[7]};
  if (c == 0) *reinterpret_cast<f32x2*>(lds + LDS_ML + (hh * TW + wave) * 2) = f32x2{m, l};
  asm volatile("s_waitcnt lgkmcnt(0)" ::: "memory");
  unsigned old = 0;
  if (lane == 0) old = __hip_atomic_fetch_add(reinterpret_cast<unsigned*>(lds) + LDS_CNT, 1u, __ATOMIC_RELAXED, __HIP_MEMORY_SCOPE_WORKGROUP);
  const unsigned arrived = (unsigned)rfl((int)old);
  asm volatile("" ::: "memory");
  if (arrived == TW - 1) {
    float ml[2 * TW];
#pragma unroll
    for (int w = 0; w < TW; ++w) {
      const f32x2 x = *reinterpret_cast<const f32x2*>(lds + LDS_ML + (hh * TW + w) * 2);
      ml[2 * w] = x[0]; ml[2 * w + 1] = x[1];
    }
    float M = MINIT;
#pragma unroll
    for (int w = 0; w < TW; ++w) M = fmaxf(M, ml[2 * w]);
    float L = 0.f, o[8];
#pragma unroll
    for (int k = 0; k < 8; ++k) o[k] = 0.f;
#pragma unroll
    for (int w = 0; w < TW; ++w) {
      const float wt = __builtin_amdgcn_exp2f(ml[2 * w] - M);
      L = fmaf(wt, ml[2 * w + 1], L);
#pragma unroll
      for (int g = 0; g < 4; ++g) {
        const f32x2 x = *reinterpret_cast<const f32x2*>(lds + LDS_ACC + w * 512 + hh * 128 + g * 32 + 2 * c);
        o[2 * g] = fmaf(wt, x[0], o[2 * g]); o[2 * g + 1] = fmaf(wt, x[1], o[2 * g + 1]);
      }
    }
    const float inv = L > 0.f ? __builtin_amdgcn_rcpf(L) : 0.f;
    const float lse = L > 0.f ? M + __builtin_amdgcn_logf(L) : MINIT;
    const unsigned slot = WS_SLOT + (unsigned)bid * SLOT_B + (unsigned)lane * 16u;
#pragma unroll
    for (int jj = 0; jj < 4; ++jj)
      __builtin_amdgcn_raw_buffer_store_b128(u32x4{u_(o[2 * jj] * inv), tag, u_(o[2 * jj + 1] * inv), tag}, rw, slot + (unsigned)(jj * 1024), 0, 16);
    __builtin_amdgcn_raw_buffer_store_b64(u32x2{u_(lse), tag}, rw, c == 0 ? WS_LSE + (unsigned)(((col * 4 + hh) * NS + s) * 8) : WS_BYTES, 0, 16);
    constexpr int NB = 8;
    static_assert(4 * NB >= NS - 1 && NS == 32, "one slice per workgroup, one batch of partials");
    const int cb = s >> 4, hd = (s >> 2) & 3, jj = s & 3;
    const int cn = cb ? NS - n0 : n0, cs0 = cb ? n0 : 0;
    const unsigned sbase = WS_SLOT + (unsigned)(col * NS + cs0) * SLOT_B;
    const unsigned lbase = WS_LSE + (unsigned)((col * 4 + hd) * NS * 8);
    const unsigned doff = (unsigned)(jj * 1024 + hd * 256 + c * 16);
    float g0[NB], g1[NB], gs[NB];
    unsigned vo[NB], lo[NB];
#pragma unroll
    for (int k = 0; k < NB; ++k) {
      const bool want = 4 * k + hh < cn;
      vo[k] = want ? (unsigned)(4 * k + hh) * SLOT_B + doff : WS_BYTES;
      lo[k] = want ? (unsigned)(cs0 + 4 * k + hh) * 8u : WS_BYTES;
      g0[k] = 0.f; g1[k] = 0.f; gs[k] = MINIT;
    }
    unsigned km = (1u << ((cn + 3) >> 2)) - 1u;
    for (unsigned spins = 0; spins < SPIN_CAP; ++spins) {
      asm volatile("" ::: "memory");
      u32x4 x[NB]; u32x2 xl[NB];
#pragma unroll
      for (int k = 0; k < NB; ++k) {
        x[k] = u32x4{0u, 0u, 0u, 0u}; xl[k] = u32x2{0u, 0u};
        if ((km >> k) & 1u) {
          x[k] = __builtin_amdgcn_raw_buffer_load_b128(rw, vo[k], sbase, 16);
          xl[k] = __builtin_amdgcn_raw_buffer_load_b64(rw, lo[k], lbase, 16);
        }
      }
#pragma unroll
      for (int k = 0; k < NB; ++k) {
        if ((km >> k) & 1u) {
          const unsigned bad = (x[k][1] ^ tag) | (x[k][3] ^ tag) | (xl[k][1] ^ tag) | (vo[k] == WS_BYTES ? 1u : 0u);
          const bool ok = bad == 0u;
          g0[k] = ok ? f_(x[k][0]) : g0[k]; g1[k] = ok ? f_(x[k][2]) : g1[k]; gs[k] = ok ? f_(xl[k][0]) : gs[k];
          vo[k] = ok ? WS_BYTES : vo[k]; lo[k] = ok ? WS_BYTES : lo[k];
          if (!__any(vo[k] != WS_BYTES)) km &= ~(1u << k);
        }
      }
      if (km == 0u) break;
      if (P_PSLEEP) __builtin_amdgcn_s_sleep(P_PSLEEP);
    }
    float Mr = MINIT, Lr = 0.f, o0 = 0.f, o1 = 0.f;
#pragma unroll
    for (int k = 0; k < NB; ++k) Mr = fmaxf(Mr, gs[k]);
    const float Mx = xrow_max(Mr);
#pragma unroll
    for (int k = 0; k < NB; ++k) {
      const float wi = __builtin_amdgcn_exp2f(gs[k] - Mx);
      Lr += wi; o0 = fmaf(wi, g0[k], o0); o1 = fmaf(wi, g1[k], o1);
    }
    const float Lt = xrow_sum(Lr), t0 = xrow_sum(o0), t1 = xrow_sum(o1);
    const float iv = 1.0f / Lt;
    if (hh == 0)
      __hip_atomic_store((gu32*)(out + (cb * H + col * 4 + hd) * D + 32 * jj + 2 * c), pack_bf16(t0 * iv, t1 * iv), __ATOMIC_RELAXED, __HIP_MEMORY_SCOPE_AGENT);
  }
  if (bid == 0 && wave == 0) {
    for (unsigned spins = 0; spins < SPIN_CAP; ++spins) {
      asm volatile("" ::: "memory");
      bool ok = true;
#pragma unroll
      for (int i = 0; i < NWG / 64; ++i) {
        const u32x2 x = __builtin_amdgcn_raw_buffer_load_b64(rw, WS_LSE + (unsigned)((((i * 64 + lane) / NS) * 4 * NS + (lane % NS)) * 8), 0, 16);
        ok &= x[1] == tag;
      }
      if (__all(ok)) break;
      __builtin_amdgcn_s_sleep(8);
    }
    if (lane == 0) __hip_atomic_store((gu32*)ws, tag, __ATOMIC_RELAXED, __HIP_MEMORY_SCOPE_AGENT);
  }
}
}

extern "C" void kernel_launch(void* const* d_in, const int* in_sizes, int n_in, void* d_out, int out_size, void* d_ws, size_t ws_size,
                              hipStream_t stream) {
  const unsigned kv_bytes = (unsigned)in_sizes[1] * 2u;
  Args a{(const unsigned short*)d_in[0], (const unsigned short*)d_in[1], (const unsigned short*)d_in[2], (const int*)d_in[3],
         (const int*)d_in[4], (unsigned short*)d_out, (unsigned char*)d_ws, kv_bytes, 0u};
  hipLaunchKernelGGL(attn_decode, dim3(NWG), dim3(NTHR), 0, stream, a);
}
```
